```python
import torch

def _softmax(x, axis=-1):
    return torch.softmax(x, dim=axis)

def _gelu(x):
    return torch.nn.functional.gelu(x, approximate='tanh')

def _silu(x):
    return torch.nn.functional.silu(x)

def _sigmoid(x):
    return torch.sigmoid(x)

def _layernorm(x, axis=-1, eps=1e-05):
    m = x.mean(dim=axis, keepdim=True)
    v = x.var(dim=axis, keepdim=True, unbiased=False)
    return (x - m) / torch.sqrt(v + eps)

def _scatter_add(tgt, idx, src, axis=0):
    return tgt.clone().index_add_(axis, idx, src)

def _segment_sum(src, seg, n):
    out = torch.zeros((n,) + src.shape[1:], dtype=src.dtype)
    return out.index_add_(0, seg, src)

def _pad(x, pw):
    flat = tuple((v for pair in reversed(pw) for v in pair))
    return torch.nn.functional.pad(x, flat)
kernel_name = 'sequential_preLN__memory_aug_recurrent_mem_tr__mixer_gmlp__prompt'

def setup_inputs():

    _w = lambda a: (lambda t: t.to(torch.bfloat16) if t.is_floating_point() else t)(torch.as_tensor(a))

    def _A_setup():
        B, S, Dm = (1, 2048, 512)
        H, Dh = (32, 128)
        M, seg = (8, 32)
        seg = min(seg, S)
        S = S // seg * seg
        return {'x': _w((lambda *s: torch.randn(*s, dtype=torch.bfloat16) * 0.05)(B, S, Dm)), 'mem0': _w((lambda *s: torch.randn(*s, dtype=torch.bfloat16) * 0.2)(B, M, Dm)), 'Wq': _w((lambda *s: torch.randn(*s, dtype=torch.bfloat16) * 1.6)(Dm, H, Dh) * Dm ** (-0.5)), 'Wk': _w((lambda *s: torch.randn(*s, dtype=torch.bfloat16) * 1.6)(Dm, H, Dh) * Dm ** (-0.5)), 'Wv': _w((lambda *s: torch.randn(*s, dtype=torch.bfloat16) * 0.2)(Dm, H, Dh) * Dm ** (-0.5)), 'Wo': _w((lambda *s: torch.randn(*s, dtype=torch.bfloat16) * 0.2)(H, Dh, Dm) * (H * Dh) ** (-0.5)), 'seg_len': seg}
    _A_in = _A_setup()

    def _B_setup():
        B, S, D, F = (1, 2048, 512, 1376)
        Fh = F // 2
        return {'x': _w((lambda *s: torch.randn(*s, dtype=torch.bfloat16) * 0.05)(B, S, D)), 'w_in': _w((lambda *s: torch.randn(*s, dtype=torch.bfloat16) * 0.4)(D, 2 * Fh)), 'w_sp': _w(0.01 * (lambda *s: torch.randn(*s, dtype=torch.bfloat16) * 0.4)(S, S)), 'b_sp': _w(1.0 + 0.01 * (lambda *s: torch.randn(*s, dtype=torch.bfloat16) * 0.05)(S)), 'w_out': _w((lambda *s: torch.randn(*s, dtype=torch.bfloat16) * 0.05)(Fh, D))}
    _B_in = _B_setup()
    return {'A_x': _A_in['x'], 'A_mem0': _A_in['mem0'], 'A_Wq': _A_in['Wq'], 'A_Wk': _A_in['Wk'], 'A_Wv': _A_in['Wv'], 'A_Wo': _A_in['Wo'], 'A_seg_len': _A_in['seg_len'], 'B_x': _B_in['x'], 'B_w_in': _B_in['w_in'], 'B_w_sp': _B_in['w_sp'], 'B_b_sp': _B_in['b_sp'], 'B_w_out': _B_in['w_out']}

def reference(A_x, A_mem0, A_Wq, A_Wk, A_Wv, A_Wo, A_seg_len, B_x, B_w_in, B_w_sp, B_b_sp, B_w_out):

    def _A_ref(x, mem0, Wq, Wk, Wv, Wo, seg_len):
        B, S, Dm = x.shape
        H, Dh = (Wq.shape[1], Wq.shape[2])
        M = mem0.shape[1]
        scale = Dh ** (-0.5)
        n_seg = S // seg_len
        L = M + seg_len
        idx = torch.arange(L)
        mask = (idx[:, None] < M) | (idx[:, None] >= idx[None, :])
        mem = mem0
        outs = []
        for s in range(n_seg):
            xs = x[:, s * seg_len:(s + 1) * seg_len, :].clone()
            h = torch.concatenate([mem, xs], axis=1)
            q = torch.einsum('btd,dhe->bhte', h, Wq)
            k = torch.einsum('btd,dhe->bhte', h, Wk)
            v = torch.einsum('btd,dhe->bhte', h, Wv)
            scores = torch.einsum('bhqe,bhke->bhqk', q, k) * scale
            scores = torch.where(mask[None, None], scores, -1000000000.0)
            p = _softmax(scores, axis=-1)
            o = torch.einsum('bhqk,bhke->bhqe', p, v)
            o = torch.einsum('bhte,hed->btd', o, Wo)
            mem = o[:, :M, :].clone()
            outs.append(o[:, M:, :])
        return torch.concatenate(outs, axis=1)

    def _B_ref(x, w_in, w_sp, b_sp, w_out):
        B, S, D = x.shape
        F = w_in.shape[-1]
        Fh = F // 2
        z = _gelu(torch.einsum('bsd,df->bsf', x, w_in))
        z1 = z[..., :Fh].clone()
        z2 = _layernorm(z[..., Fh:], axis=-1)
        g = torch.einsum('ts,bsf->btf', w_sp, z2) + b_sp[None, :, None]
        return torch.einsum('bsf,fd->bsd', z1 * g, w_out)
    x = A_x
    h = x + _A_ref(_layernorm(x, axis=-1), A_mem0, A_Wq, A_Wk, A_Wv, A_Wo, A_seg_len)
    h2 = h + _B_ref(_layernorm(h, axis=-1), B_w_in, B_w_sp, B_b_sp, B_w_out)
    return h2
```

```cpp
#include <hip/hip_runtime.h>
#include <hip/hip_bf16.h>
#include <stdint.h>

typedef unsigned u32;
typedef unsigned short u16;
typedef __attribute__((ext_vector_type(2))) unsigned u2;
typedef __attribute__((ext_vector_type(4))) unsigned u4;
typedef __attribute__((ext_vector_type(8))) unsigned u8;
typedef __attribute__((ext_vector_type(2))) float f2;
typedef __attribute__((ext_vector_type(4))) float f4;
typedef __attribute__((ext_vector_type(2))) __bf16 b2;
typedef __attribute__((ext_vector_type(8))) __bf16 b8;
typedef __attribute__((ext_vector_type(16))) __bf16 b16;
typedef __attribute__((ext_vector_type(4))) short s4;
typedef __amdgpu_buffer_rsrc_t rs_t;
#define DEV __device__ __forceinline__
#define SB() __builtin_amdgcn_sched_barrier(0)
#define BAR() asm volatile("s_waitcnt lgkmcnt(0)\n\ts_barrier" ::: "memory")
#define VM0() asm volatile("s_waitcnt vmcnt(0)" ::: "memory")
#define SC1 16
#define MB (1u << 20)
#define W_IN 0u
#define W_IN2 (3 * MB)
#define W_MS (2 * MB)
#define W_TF (108 * MB)
#define W_HF (108 * MB + 8192)
#define W_SEL (108 * MB + 4096)
#define W_PF (108 * MB + 5120)
#define W_NF (108 * MB + 6144)
#define W_ZF (108 * MB + 7168)
#define W_XN (4 * MB)
#define W_H (6 * MB)
#define W_Q (8 * MB)
#define W_K (24 * MB)
#define W_V (40 * MB)
#define W_KM (56 * MB)
#define W_VM (60 * MB)
#define W_P (64 * MB)
#define W_HN (80 * MB)
#define W_WU (100 * MB)
#define W_Z1 (96 * MB)
#define W_Z2 (84 * MB)
#define W_U (104 * MB)
#define W_END (110 * MB)
#define SENT 0xFFFFFFFFu

#define K0P 0
#define MSP 1
#define MO2 2048
DEV u32 mslot(u32 s) { return W_MS + (s >> 1) * 32768 + (s & 1) * 128; }
DEV u32 mline(u32 i) { return (i & 31) * 256 + (i >> 5) * 16384; }
DEV rs_t mk(const void* p, u32 n) { return __builtin_amdgcn_make_buffer_rsrc((void*)p, 0, n, 0x00020000); }
DEV rs_t mkws(void* ws) {
  uintptr_t a = ((uintptr_t)ws + (2 * MB - 1)) & ~(uintptr_t)(2 * MB - 1);
  return mk((void*)a, W_END);
}
template <int X> DEV u4 ld16(rs_t r, u32 v, u32 s) { return __builtin_amdgcn_raw_buffer_load_b128(r, v, s, X); }
template <int X> DEV u2 ld8(rs_t r, u32 v, u32 s) { return __builtin_amdgcn_raw_buffer_load_b64(r, v, s, X); }
template <int X> DEV u32 ld4(rs_t r, u32 v, u32 s) { return __builtin_amdgcn_raw_buffer_load_b32(r, v, s, X); }
DEV u32 ld2(rs_t r, u32 v, u32 s) { return __builtin_amdgcn_raw_buffer_load_b16(r, v, s, 0); }
template <int X> DEV void st16u(u4 d, rs_t r, u32 v, u32 s) { __builtin_amdgcn_raw_buffer_store_b128(d, r, v, s, X); }
template <int X, bool PIN = false> DEV void st16(u4 d, rs_t r, u32 v, u32 s) {
  if (PIN) asm volatile("" : "+v"(d));
  st16u<X>(d, r, v, s);
  asm volatile("s_nop 1" ::"v"(d) : "memory");
}
template <int X> DEV void st8(u2 d, rs_t r, u32 v, u32 s) { __builtin_amdgcn_raw_buffer_store_b64(d, r, v, s, X); }
template <int X> DEV void st4(u32 d, rs_t r, u32 v, u32 s) { __builtin_amdgcn_raw_buffer_store_b32(d, r, v, s, X); }
DEV u32 pk(float a, float b) {
  f2 v = {a, b};
  return __builtin_bit_cast(u32, __builtin_convertvector(v, b2));
}
DEV float lo(u32 x) { return __builtin_bit_cast(float, x << 16); }
DEV float hi(u32 x) { return __builtin_bit_cast(float, x & 0xffff0000u); }
DEV u32 plo(u32 r0, u32 r1) { return __builtin_amdgcn_perm(r1, r0, 0x05040100u); }
DEV u32 phi(u32 r0, u32 r1) { return __builtin_amdgcn_perm(r1, r0, 0x07060302u); }
DEV f4 mm(u4 a, u4 b, f4 c) {
  return __builtin_amdgcn_mfma_f32_16x16x32_bf16(__builtin_bit_cast(b8, a), __builtin_bit_cast(b8, b), c, 0, 0, 0);
}
DEV f4 mm16(u32 a, u32 b, f4 c) {
  u2 x = {a, 0u}, y = {b, 0u};
  return __builtin_amdgcn_mfma_f32_16x16x16bf16_1k(__builtin_bit_cast(s4, x), __builtin_bit_cast(s4, y), c, 0, 0, 0);
}
DEV f4 sm(u4 a, u8 b, f4 c, u32 idx) {
  return __builtin_amdgcn_smfmac_f32_16x16x64_bf16(__builtin_bit_cast(b8, a), __builtin_bit_cast(b16, b), c, idx, 0, 0);
}
DEV u32 fu(float x) { return __builtin_bit_cast(u32, x); }
DEV float uf(u32 x) { return __builtin_bit_cast(float, x); }
DEV float fmax2(float a, float b) {
  return __builtin_fmaxf(a, b);
}
DEV float fmax3(float a, float b, float c) { return fmax2(fmax2(a, b), c); }
DEV float gmax(float x) {
  auto a = __builtin_amdgcn_permlane32_swap(fu(x), fu(x), false, false);
  x = fmax2(uf(a[0]), uf(a[1]));
  auto c = __builtin_amdgcn_permlane16_swap(fu(x), fu(x), false, false);
  return fmax2(uf(c[0]), uf(c[1]));
}
DEV float gsum(float x) {
  auto a = __builtin_amdgcn_permlane32_swap(fu(x), fu(x), false, false);
  x = uf(a[0]) + uf(a[1]);
  auto c = __builtin_amdgcn_permlane16_swap(fu(x), fu(x), false, false);
  return uf(c[0]) + uf(c[1]);
}
template <int C> DEV float dpp(float x) { return uf(__builtin_amdgcn_update_dpp(0, fu(x), C, 0xf, 0xf, true)); }
DEV float rsum(float x) {
  x += dpp<0xB1>(x);
  x += dpp<0x4E>(x);
  x += dpp<0x141>(x);
  x += dpp<0x140>(x);
  return x;
}
DEV u32 umax3(u32 a, u32 b, u32 c) { return max(max(a, b), c); }
DEV bool fresh(u4 a, u4 b) {
  u32 m = max(umax3(a[0], a[1], a[2]), umax3(umax3(a[3], b[0], b[1]), b[2], b[3]));
  return __builtin_amdgcn_ballot_w64(m == SENT) == 0;
}
DEV u4 lnrow(u4 v) {
  float x[8];
#pragma unroll
  for (int i = 0; i < 4; i++) { x[2 * i] = lo(v[i]); x[2 * i + 1] = hi(v[i]); }
  float s = 0;
#pragma unroll
  for (int i = 0; i < 8; i++) s += x[i];
  float mu = gsum(rsum(s)) * (1.f / 512);
  s = 0;
#pragma unroll
  for (int i = 0; i < 8; i++) { x[i] -= mu; s += x[i] * x[i]; }
  float rs = __builtin_amdgcn_rsqf(gsum(rsum(s)) * (1.f / 512) + 1e-5f);
  u4 o;
#pragma unroll
  for (int i = 0; i < 4; i++) o[i] = pk(x[2 * i] * rs, x[2 * i + 1] * rs);
  return o;
}

__global__ __launch_bounds__(256) void k0(const void* x, const void* mem0, const void* wq, const void* wk, const void* wv, const void* wo,
                                          const void* win, const void* wout, void* ws) {
  const int tid = threadIdx.x, b = blockIdx.x, w = tid >> 6, l = tid & 63;
  rs_t R = mkws(ws), X = mk(x, 2 * MB);
  u4 v0 = ld16<0>(X, l * 16, (b * 8 + w * 2) * 1024), v1 = ld16<0>(X, l * 16, (b * 8 + w * 2 + 1) * 1024);
  u32 gt = b * 256 + tid;
  const u32 po = ((b >> 1) * 256 + tid) * 128;
  const u32 p0 = K0P ? ld4<0>(mk((b & 1) ? wk : wq, 4 * MB), po, 0) : 0, p1 = K0P ? ld4<0>(mk((b & 1) ? wo : wv, 4 * MB), po, 0) : 0;
  const u32 p2 = ld4<0>((b & 1) ? mk(wout, 688 * 1024) : mk(win, 512 * 2752), po, 0);
  u4 se = {SENT, SENT, SENT, SENT};
  st16<SC1, true>(se, R, gt * 16, 0);
  st16<SC1, true>(se, R, gt * 16, MB);
  const u32 ln = MSP == 0 ? (gt >> 3) & 63 : MSP == 1 ? ((gt >> 4) & 31) + ((gt >> 10) & 1) * 32 : ((gt >> 4) & 15) + ((gt >> 10) & 3) * 16;
  if (((gt * 16) & ~8319u) == mline(ln)) {
    u32 p = ln * 8 + (gt & 7), i = (p & 1) + 2 * ((p >> 3) & 3), d = (p >> 5) * 32 + ((p >> 1) & 3) * 8;
    se = ld16<0>(mk(mem0, 8192), i * 1024 + d * 2, 0);
#pragma unroll
    for (int j = 0; j < 4; j++) se[j] = min(se[j], SENT - 1);
  }
  if (b >= 32 && b < 36) {
    u4 z = {0, 0, 0, 0};
    st16<SC1, true>(z, R, (gt - 8192) * 16, W_TF);
  }
  st16<SC1, true>(se, R, gt * 16, W_MS);
  int tok = b * 8 + w * 2;
  u32 o = (tok >> 5) * 32768 + (l >> 2) * 2048 + ((tok >> 4) & 1) * 1024 + ((l & 3) * 16 + (tok & 15)) * 16;
  st16<0, true>(lnrow(v0), R, o, W_XN);
  st16<0, true>(lnrow(v1), R, o + 16, W_XN);
  asm volatile("" ::"v"(p0), "v"(p1), "v"(p2));
}
#define XA 144
#define XB 48
#define B4W 0
#define XT 50
#define QSC 0
#define EXP 0
#define EA 8
#define EB 2
#define XQ 36
#define XH 18
#define PD 0
#define PFL 0
#define PR 0
#define PR2 0
#define NS0 ((PD == 0) * 3 + (PFL == 0) + (PR == 0) + (PR2 == 0))
#define SD 2
#define PNEAR 0x1f00
#define SLS 32
#define SLF 3
#define SLP 8
#define TDL 0
typedef __attribute__((ext_vector_type(16))) unsigned u16v;
#define STR_(x) #x
#define STR(x) STR_(x)
#define PINA(x) asm volatile("" : "+a"(x))
#define PINV(x) asm volatile("" : "+v"(x))
#define PIN2(x, y) asm volatile("" : "+v"(x), "+v"(y))
#define NOP(n) asm volatile("s_nop " #n)
#define SMA(d, a, b) asm volatile("v_smfmac_f32_16x16x64_bf16 %0, %1, %2, %3" : "+v"(d) : "v"(a), "a"(b), "v"(idx))
#define SMV(d, a, b) asm volatile("v_smfmac_f32_16x16x64_bf16 %0, %1, %2, %3" : "+v"(d) : "v"(a), "v"(b), "v"(idx))
#define MMZ(d, a, b) asm volatile("v_mfma_f32_16x16x32_bf16 %0, %1, %2, 0" : "=&v"(d) : "v"(a), "v"(b))
#define MMA(d, a, b) asm volatile("v_mfma_f32_16x16x32_bf16 %0, %1, %2, %0" : "+v"(d) : "v"(a), "v"(b))
#define MWA(d, a, b) asm volatile("v_mfma_f32_16x16x32_bf16 %0, %1, %2, %0" : "+v"(d) : "a"(a), "v"(b))
#define MWB(d, a, b) asm volatile("v_mfma_f32_16x16x32_bf16 %0, %1, %2, %0" : "+v"(d) : "v"(a), "a"(b))
#define WAIT4(x) asm volatile("s_nop 7" : "+v"(x[0]), "+v"(x[1]), "+v"(x[2]), "+v"(x[3]))
DEV f4 zero4() {
  f2 a, b;
  asm volatile("v_mov_b64 %0, 0\n\tv_mov_b64 %1, 0" : "=v"(a), "=v"(b));
  return f4{a[0], a[1], b[0], b[1]};
}
DEV f2 fold(f4 d) {
  f2 r, a = {d[0], d[1]}, b = {d[2], d[3]};
  asm volatile("v_pk_add_f32 %0, %1, %2" : "=v"(r) : "v"(a), "v"(b));
  return r;
}
DEV u32 cvt(float a, float b) {
  u32 r;
  asm volatile("v_cvt_pk_bf16_f32 %0, %1, %2" : "=v"(r) : "v"(a), "v"(b));
  return r;
}
DEV float vmax3(float a, float b, float c) {
  float r;
  asm volatile("v_max3_f32 %0, %1, %2, %3" : "=v"(r) : "v"(a), "v"(b), "v"(c));
  return r;
}
#define TS(i)
#define DBG 0
#define RT(c, i)
#define CNT
#define DBT
#define CEXP 0.12751743f

DEV u4 desc(const void* p, u32 n) {
  const uintptr_t a = (uintptr_t)p;
  return u4{(u32)a, (u32)(a >> 32) & 0xffffu, n, 0x00020000u};
}
DEV void dma1(u4 D, u32 lds, u32 v, u32 so) {
  asm volatile("s_mov_b32 m0, %0\n\ts_nop 0\n\tbuffer_load_dwordx4 %1, %2, %3 offen sc1 lds" ::"s"(lds), "v"(v), "s"(D), "s"(so) : "memory");
}
#define PINS(x) asm volatile("" : "+s"(x))
DEV void stage(u4 D, u32 lds, u32 v, u32 so, u32 st1, u32 st2) {
#pragma unroll
  for (int i = 0; i < 16; i++)
    asm volatile("s_mov_b32 m0, %0\n\ts_nop 0\n\tbuffer_load_dwordx4 %1, %2, %3 offen lds" ::"s"(lds + i * 4096), "v"(v), "s"(D),
                 "s"(so + (i & 1) * st1 + (i >> 1) * st2) : "memory");
}
typedef __attribute__((address_space(3))) s4* ltr_t;
DEV u2 trd(u32 a) { return __builtin_bit_cast(u2, __builtin_amdgcn_ds_read_tr16_b64_v4i16((ltr_t)(uintptr_t)a)); }
#define VMW(n) asm volatile("s_waitcnt vmcnt(" #n ")" ::: "memory")
DEV u32 avail_of(rs_t R, int h) {
  asm volatile("" ::: "memory");
  u4 f = ld16<SC1>(R, 0, W_TF + h * 128);
  return __builtin_amdgcn_readfirstlane(min(min(f[0] * 4, f[1] * 4 + 1), min(f[2] * 4 + 2, f[3] * 4 + 3)));
}

template <int SLOW> DEV void waitall(rs_t R, int w, u32 v, u32 a, u32 j, u32& f, bool fast, int& budget) {
  if (w == 0 && __builtin_amdgcn_ballot_w64(f <= j) != 0) {
    for (;;) {
      asm volatile("" ::: "memory");
      f = ld4<SC1>(R, v, a);
      if (__builtin_amdgcn_ballot_w64(f <= j) == 0 || --budget <= 0) break;
      if (fast) __builtin_amdgcn_s_sleep(SLF); else __builtin_amdgcn_s_sleep(SLOW);
    }
  }
  BAR();
}

DEV u32 half(u32 base, u32 k, u32 sel) {
  const u32 a = base + (k >> 3) * 16384 + (k & 7) * 1024;
  return a + ((sel >> ((a - W_P) >> 21)) & 1) * 8192;
}
DEV u32 getsel(rs_t R, int& budget) {
  u32 sel = 0;
  while (sel == 0 && --budget > 0) {
    asm volatile("" ::: "memory");
    sel = __builtin_amdgcn_readfirstlane(ld4<SC1>(R, 0, W_SEL + 512));
    if (sel == 0) __builtin_amdgcn_s_sleep(64);
  }
  return sel;
}

DEV void hcu(rs_t R, u4 RD, u4 WQ, u4 WK, u4 WV, rs_t WO, char* sh, int h, int c, int w, int l) {
  u4* mb = (u4*)sh;
  const u32 lb = (u32)(uintptr_t)(__attribute__((address_space(3))) char*)sh;
  const int g = l >> 4, n = l & 15, tid = w * 64 + l, pid = h * 2 + c;
  u32* qw = (u32*)(sh + 16384 + w * 1088 + (n >> 3) * 544 + ((n >> 2) & 1) * 256 + g * 64 + (n & 3) * 4);
  const char* qr = sh + 16384 + (g >> 1) * 544 + (g & 1) * 256 + n * 16;
  u2* ow = (u2*)(sh + 20736 + w * 256 + ((n & 13) + (g & 2)) * 16 + (g & 1) * 8 + ((n & 2) ? 0 : 1056));
  u8 wq[16], wk[16], wv[8], wo[8];
  {
    const u32 dr = w * 4 + g, gv = dr * 8192 + ((n ^ (4 * ((dr & 1) + 2 * (dr >> 3)))) * 16);
    const u32 dv = w * 8 + (l >> 3), gv2 = dv * 8192 + (((l & 7) ^ (4 * ((dv >> 3) & 1))) * 16);
    const u32 r = n >> 2, rowl = 16 * (g >> 1) + 4 * (g & 1) + 8 * (r >> 1) + (r & 1);
    const u32 ta = lb + rowl * 256 + (((4 * w + (n & 3)) ^ (4 * r)) * 16);
    const u32 tv = lb + rowl * 128 + (((4 * (w >> 1) + (n & 3)) ^ (4 * (r >> 1))) * 16) + (w & 1) * 8;
    stage(WQ, lb + w * 1024, gv, h * 256, 16 * 8192, 64 * 8192);
#pragma unroll
    for (int m = 0; m < 5; m++) {
      const u32 nb = lb + ((m + 1) & 1) * 65536 + w * 1024;
      if (m < 3) stage(m == 0 ? WQ : WK, nb, gv, h * 256 + ((m + 1) & 1) * 32 * 8192, 16 * 8192, 64 * 8192);
      if (m == 3) stage(WV, nb, gv2, h * 256 + c * 128, 32 * 8192, 64 * 8192);
      if (m < 4) {
        VMW(16);
      } else {
#pragma unroll
        for (int jj = 0; jj < 8; jj++) {
          const u32 s0 = (pid * 64 + 32 * (jj >> 2) + 16 * (jj & 1) + 2 * ((jj >> 1) & 1)) * 1024 + w * 256;
          const u32 v0 = (8 * (g & 1) + 4 * (g >> 1)) * 1024 + n * 8;
#pragma unroll
          for (int x = 0; x < 2; x++) {
            const u2 a = ld8<0>(WO, v0, s0 + x * 128), b = ld8<0>(WO, v0, s0 + x * 128 + 1024);
#pragma unroll
            for (int t = 0; t < 2; t++) wo[4 * x + 2 * t][jj] = plo(a[t], b[t]), wo[4 * x + 2 * t + 1][jj] = phi(a[t], b[t]);
          }
        }
#pragma unroll
        for (int t = 0; t < 8; t++) PINV(wo[t]);
        VM0();
      }
      BAR();
#pragma unroll
      for (int j = 0; j < 8; j++) {
        const u32 kb = (2 * w + j) & 7;
#pragma unroll
        for (int y = 0; y < 4; y++) {
          if (m < 4) {
            const u2 t = trd(ta + (m & 1) * 65536 + kb * 8192 + (y >> 1) * 512 + (y & 1) * 8);
            u8& d = m < 2 ? wq[2 * j + (y & 1)] : wk[2 * j + (y & 1)];
            d[4 * (m & 1) + (y & 2)] = t[0], d[4 * (m & 1) + (y & 2) + 1] = t[1];
          } else {
            const u2 t = trd(tv + kb * 8192 + (y >> 1) * 4096 + (y & 1) * 256);
            wv[j][2 * y] = t[0], wv[j][2 * y + 1] = t[1];
          }
        }
        if (m == 1) { PINA(wq[2 * j]); PINA(wq[2 * j + 1]); }
        if (m == 3) { PINA(wk[2 * j]); PINA(wk[2 * j + 1]); }
        if (m == 4) PINV(wv[j]);
      }
      BAR();
    }
  }
  const u32 idx = (l & 2) ? 0xEEEEu : 0x4444u;
  const u32 mvo = mline(w * 16 + (g >> 1) * 4 + (n >> 2)) + (g & 1) * 64 + (n & 3) * 16;
  const u32 lam = 16 * w + (n >> 3) * 4 + g;
  const u32 pv0 = lam * 16384 + ((pid + 2 * lam) & 63) * 128 + (n & 7) * 16;
  const u32 pv1 = (lam + 8) * 16384 + ((pid + 2 * lam + 16) & 63) * 128 + (n & 7) * 16;
  const u32 one = (n & 1) ? 0x3F800000u : 0x3F80u, sp = (n & 7) >> 1;
  const u32 fvo = tid == 0 ? 0u : 0x80000000u;
  const u32 bvo = (l & 40) == 0 ? (n >> 1) * 32 + g * 16 + (n & 1) * 8 : 0x80000000u;
  u4 pm = {0, 0, 0, 0}, ones = {0x3F803F80u, 0x3F803F80u, 0x3F803F80u, 0x3F803F80u};
  PIN2(pm, ones);
  u4 selB = {sp == 0 ? one : 0u, sp == 1 ? one : 0u, sp == 2 ? one : 0u, sp == 3 ? one : 0u};
  const u4 sent = {SENT, SENT, SENT, SENT};
  PINV(selB);
  const f2 cex = {CEXP, CEXP};
  TS(0);
  int budget = 1 << 16;
  u32 avail = 0;
  while (avail <= 0 && --budget > 0) { avail = avail_of(R, h); __builtin_amdgcn_s_sleep(4); }
  u32 sel = 0, tk = 0;
  while (sel == 0 && --budget > 0) {
    asm volatile("" ::: "memory");
    const u2 f = ld8<SC1>(R, 0, W_SEL);
    sel = __builtin_amdgcn_readfirstlane(f[0]), tk = __builtin_amdgcn_readfirstlane(f[1]);
  }
  if ((u32)__builtin_amdgcn_s_memrealtime() - tk > 65536u) tk = (u32)__builtin_amdgcn_s_memrealtime();
  const u32 xa = XA, xb = XB;
  const u32 selI = (sel & 2) * 4096, selM = (sel & 4) * 2048;
  dma1(RD, lb + 24576 + w * 2048, l * 16, W_K + h * 8192 + w * 2048);
  dma1(RD, lb + 24576 + w * 2048 + 1024, l * 16, W_K + h * 8192 + w * 2048 + 1024);
  dma1(RD, lb + 24576 + 8192 + w * 1024, l * 16, W_V + h * 8192 + (4 * c + w) * 1024);
  TS(1);
#define SLOT(e) "s_memrealtime s[98:99]\n\ts_sub_i32 %[t], %[t], " STR(XQ) "\n\ts_waitcnt lgkmcnt(0)\n5:\n\ts_add_i32 %[t], %[t], " STR(XQ) "\n\t" \
  "s_sub_i32 %[d], s98, %[t]\n\ts_cmp_gt_i32 %[d], " STR(e) "\n\ts_cbranch_scc1 5b\n" \
  "4:\n\ts_memrealtime s[98:99]\n\ts_waitcnt lgkmcnt(0)\n\ts_sub_i32 %[d], s98, %[t]\n\ts_cmp_lt_i32 %[d], 0\n\ts_cbranch_scc1 4b\n\t"
#define LDA "buffer_load_dwordx4 v[216:219], %[lo], %[rd], %[a0] offen offset:0 sc1\n\t" \
  "buffer_load_dwordx4 v[220:223], %[lo], %[rd], %[a0] offen offset:1024 sc1\n\t" \
  "buffer_load_dwordx4 v[224:227], %[lo], %[rd], %[a0] offen offset:2048 sc1\n\t" \
  "buffer_load_dwordx4 v[228:231], %[lo], %[rd], %[a0] offen offset:3072 sc1\n\t" \
  "buffer_load_dwordx4 v[232:235], %[lo], %[rd], %[a1] offen offset:0 sc1\n\t" \
  "buffer_load_dwordx4 v[236:239], %[lo], %[rd], %[a1] offen offset:1024 sc1\n\t" \
  "buffer_load_dwordx4 v[240:243], %[lo], %[rd], %[a1] offen offset:2048 sc1\n\t" \
  "buffer_load_dwordx4 v[244:247], %[lo], %[rd], %[a1] offen offset:3072 sc1\n\t" \
  ""
#define RED "s_waitcnt vmcnt(7)\n\tv_mfma_f32_16x16x32_bf16 v[248:251], %[sel], v[216:219], 0\n\tv_max_u32 v252, v216, v217\n\tv_max3_u32 v252, v252, v218, v219\n\t" \
  "s_waitcnt vmcnt(6)\n\tv_mfma_f32_16x16x32_bf16 v[248:251], %[sel], v[220:223], v[248:251]\n\tv_max3_u32 v252, v252, v220, v221\n\tv_max3_u32 v252, v252, v222, v223\n\t" \
  "s_waitcnt vmcnt(5)\n\tv_mfma_f32_16x16x32_bf16 v[248:251], %[sel], v[224:227], v[248:251]\n\tv_max3_u32 v252, v252, v224, v225\n\tv_max3_u32 v252, v252, v226, v227\n\t" \
  "s_waitcnt vmcnt(4)\n\tv_mfma_f32_16x16x32_bf16 v[248:251], %[sel], v[228:231], v[248:251]\n\tv_max3_u32 v252, v252, v228, v229\n\tv_max3_u32 v252, v252, v230, v231\n\t" \
  "s_waitcnt vmcnt(3)\n\tv_mfma_f32_16x16x32_bf16 v[248:251], %[sel], v[232:235], v[248:251]\n\tv_max3_u32 v252, v252, v232, v233\n\tv_max3_u32 v252, v252, v234, v235\n\t" \
  "s_waitcnt vmcnt(2)\n\tv_mfma_f32_16x16x32_bf16 v[248:251], %[sel], v[236:239], v[248:251]\n\tv_max3_u32 v252, v252, v236, v237\n\tv_max3_u32 v252, v252, v238, v239\n\t" \
  "s_waitcnt vmcnt(1)\n\tv_mfma_f32_16x16x32_bf16 v[248:251], %[sel], v[240:243], v[248:251]\n\tv_max3_u32 v252, v252, v240, v241\n\tv_max3_u32 v252, v252, v242, v243\n\t" \
  "s_waitcnt vmcnt(0)\n\tv_mfma_f32_16x16x32_bf16 v[248:251], %[sel], v[244:247], v[248:251]\n\tv_max3_u32 v252, v252, v244, v245\n\tv_max3_u32 v252, v252, v246, v247\n\t" \
  ""
#define RREGS(m) m "{v[216:231]}"(ra), m "{v[232:247]}"(rb)
  u32 sr, sr2, sk, sk2, sv, lk, lk2, lv, sm0, spk, spv, si = W_IN2 + selM, i1 = W_IN + selI, i2 = i1 + MB, sb, sa0, sa1, sfl, kxo, vxo, td;
#define PRE(s)                                                                                                      \
  {                                                                                                                 \
    const u32 sn = min((s) + 1, 63);                                                                                \
    while (__builtin_expect(avail <= sn, 0) && --budget > 0) avail = avail_of(R, h);                                \
    sr = si + pid * 16384, sr2 = sr + 4096, td = si, si = i1, i1 = i2, i2 = td;             \
    sk = W_K + (sn * 32 + h) * 8192 + w * 2048, sk2 = sk + 1024, sv = W_V + (sn * 32 + h) * 8192 + (4 * c + w) * 1024; \
    lk = lb + 24576 + (((s) + 1) & 1) * 12288 + w * 2048, lk2 = lk + 1024, lv = lk + 8192 - w * 1024;               \
    sm0 = mslot(s) + selM, spk = W_KM + ((s) * 32 + h) * 2048, spv = spk + (W_VM - W_KM) + (4 * c + w) * 256;       \
    sb = sm0 + mline(pid), sa0 = si + pid * 16384, sa1 = sa0 + 4096, sfl = s;    \
    kxo = 24576 + ((s) & 1) * 12288 + l * 16, vxo = kxo + 8192 + w * 1024;                                          \
    PINS(sr); PINS(sr2); PINS(sk); PINS(sk2); PINS(sv); PINS(lk); PINS(lk2); PINS(lv); PINS(sm0);                   \
    PINS(spk); PINS(spv); PINS(si); PINS(sb); PINS(sa0); PINS(sa1); PINV(sfl); PIN2(kxo, vxo);                      \
  }
  if (w != 1) avail = 64;
  PRE(0);
  VM0();
  asm volatile(SLOT(0) : [t] "+s"(tk), [d] "=&s"(td)::"s98", "s99", "scc");
  for (int s = 0;; s++) {
    u4 mf[8];
    const u4* kxb = (const u4*)(sh + kxo);
    TS(2);
#define SIDE(p)                                                                     \
  SB();                                                                             \
  if (PD == p) { dma1(RD, lk, l * 16, sk); dma1(RD, lk2, l * 16, sk2); dma1(RD, lv, l * 16, sv); } \
  if (PFL == p) st4<SC1>(sfl, R, fvo, W_HF + pid * 128);   \
  if (PR == p) st16u<SC1>(sent, R, tid * 16, sr);                                    \
  if (PR2 == p) st16u<SC1>(sent, R, tid * 16, sr2);                                  \
  SB();
#define LDB "buffer_load_dwordx4 v[248:251], %[v], %[rd], %[a] offen sc1\n\tbuffer_load_dwordx4 v[252:255], %[v], %[rd], %[a] offen offset:" STR(MO2) " sc1\n\ts_memrealtime s[96:97]\n\t"
    u32 t0, t1;
    u2 tb;
    asm volatile("s_barrier\n\t" LDB : "=&{v[248:251]}"(mf[0]), "=&{v[252:255]}"(mf[1]), "=&{s[96:97]}"(tb) : [v] "v"(mvo), [rd] "s"(RD), [a] "s"(sm0) : "memory");
    f4 q0 = zero4(), q1 = zero4(), k0 = zero4(), k1 = zero4(), va = zero4(), vb = zero4();
    if (SD) __builtin_amdgcn_s_sleep(SD);
    SIDE(0);
    asm volatile("s_branch 6f\n2:\n\ts_sub_i32 %[b], %[b], 1\n\ts_cmp_gt_i32 %[b], 0\n\ts_cbranch_scc0 9f\n4:\n\ts_memrealtime s[98:99]\n\ts_waitcnt lgkmcnt(0)\n\t"
                 "s_sub_i32 s98, s98, s96\n\ts_cmp_lt_u32 s98, " STR(XQ) "\n\ts_cbranch_scc1 4b\n\t" LDB "s_waitcnt vmcnt(0)\n"
                 "6:\n\ts_waitcnt vmcnt(%[n1]) lgkmcnt(0)\n\tv_max3_u32 %[t0], v248, v249, v250\n\ts_waitcnt vmcnt(%[n0])\n\tv_max3_u32 %[t1], v251, v252, v253\n\t"
                 "v_max3_u32 %[t0], %[t0], v254, v255\n\tv_max_u32 %[t0], %[t0], %[t1]\n\tv_cmp_eq_u32 vcc, -1, %[t0]\n\ts_cbranch_vccnz 2b\n9:"
                 : "+{v[248:251]}"(mf[0]), "+{v[252:255]}"(mf[1]), "+{s[96:97]}"(tb), [t0] "=&v"(t0), [t1] "=&v"(t1), [b] "+s"(budget)
                 : [v] "v"(mvo), [rd] "s"(RD), [a] "s"(sm0), [n0] "n"(NS0), [n1] "n"(NS0 + 1) : "memory", "vcc", "scc", "s98", "s99");
    TS(3);
    mb[w * 128 + l] = mf[0];
    mb[w * 128 + 64 + l] = mf[1];
    SB();
    SMA(q0, mf[0], wq[0]); SMA(q1, mf[0], wq[1]); SMA(q0, mf[1], wq[2]); SMA(q1, mf[1], wq[3]);
    BAR();
    asm volatile("s_memrealtime s[96:97]" : "={s[96:97]}"(tb));
    TS(10);
#pragma unroll
    for (int j = 2; j < 8; j++) mf[j] = (mb + ((w + j / 2) & 3) * 128 + l)[(j & 1) * 64];
    SB();
    SMA(k0, mf[0], wk[0]); SMA(k1, mf[0], wk[1]); SMA(k0, mf[1], wk[2]); SMA(k1, mf[1], wk[3]);
    SMV(va, mf[0], wv[0]); SMV(vb, mf[1], wv[1]);
    SIDE(1);
    SB();
#pragma unroll
    for (int j = 2; j < 8; j++) {
      SMA(q0, mf[j], wq[2 * j]);
      SMA(q1, mf[j], wq[2 * j + 1]);
    }
    u4 qf[4], kx[8];
    f2 x0, x1;
#pragma unroll
    for (int j = 2; j < 8; j++) {
      SMA(k0, mf[j], wk[2 * j]);
      SB();
      if (j < 6) kx[2 * j - 4] = kxb[(2 * j - 4) * 64];
      if (j == 4) x0 = fold(q0);
      if (j == 5) asm volatile("v_pk_mul_f32 %0, %0, %1" : "+v"(x0) : "s"(cex));
      if (j == 6) qw[8] = cvt(x0[0], x1[0]);
      SB();
      SMA(k1, mf[j], wk[2 * j + 1]);
      SB();
      if (j < 6) kx[2 * j - 3] = kxb[(2 * j - 3) * 64];
      if (j == 4) x1 = fold(q1);
      if (j == 5) asm volatile("v_pk_mul_f32 %0, %0, %1" : "+v"(x1) : "s"(cex));
      if (j == 6) qw[12] = cvt(x0[1], x1[1]);
      SB();
    }
    NOP(2); SMV(va, mf[2], wv[2]); NOP(2); SMV(vb, mf[3], wv[3]);
    x0 = fold(k0), x1 = fold(k1);
    const u32 kd0 = cvt(x0[0], x1[0]), kd1 = cvt(x0[1], x1[1]);
    qw[0] = kd0;
    qw[4] = kd1;
    SB();
    SMV(va, mf[4], wv[4]); SMV(vb, mf[5], wv[5]);
    u32 kpub = c ? kd1 : kd0;
    PINV(kpub);
    SIDE(2);
    BAR();
    TS(11);
#pragma unroll
    for (int i = 0; i < 4; i++) qf[i] = *(const u4*)(qr + i * 1088);
    SB();
    SMV(va, mf[6], wv[6]); SMV(vb, mf[7], wv[7]);
    u4 vx = *(const u4*)(sh + vxo);
    SIDE(3);
    st4<SC1>(kpub, R, (2 * g + c) * 256 + w * 64 + n * 4, spk);
    SB();
    NOP(2);
    f4 s0, s1, sa;
    MMZ(s0, kx[0], qf[0]); MMZ(s1, kx[1], qf[0]); MMA(s0, kx[2], qf[1]);
    x0 = fold(va), x1 = fold(vb);
    MMA(s1, kx[3], qf[1]);
    asm volatile("v_pk_add_f32 %0, %0, %1" : "+v"(x0) : "v"(x1));
    MMA(s0, kx[4], qf[2]);
    const u32 vd = cvt(x0[0], x0[1]);
    MMA(s1, kx[5], qf[2]);
    SB();
    st4<SC1>(vd, R, l * 4, spv);
    if (s == 63) break;
    SB();
    MMA(s0, kx[6], qf[3]); MMA(s1, kx[7], qf[3]);
    NOP(2); MMZ(sa, qf[0], qf[0]); NOP(2); MMA(sa, qf[1], qf[1]);
    float mx = vmax3(s0[0], s0[1], s0[2]), my = vmax3(s1[0], s1[1], s1[2]);
    MMA(sa, qf[2], qf[2]);
    mx = vmax3(mx, s0[3], s1[3]);
    MMA(sa, qf[3], qf[3]);
    f4 d[8];
#pragma unroll
    for (int t = 0; t < 4; t++) d[t] = zero4();
    PINV(sa);
    my = vmax3(my, sa[0], sa[1]);
    float mt;
    asm volatile("v_max_f32 %1, %0, %2\n\tv_max_f32 %0, %0, %2\n\ts_nop 1\n\tv_permlane32_swap_b32 %0, %1\n\t"
                 "v_max_f32 %2, %0, %1\n\tv_max_f32 %0, %0, %1\n\ts_nop 1\n\tv_permlane16_swap_b32 %0, %2\n\t"
                 "v_max_f32 %0, %0, %2"
                 : "+v"(mx), "=&v"(mt), "+v"(my));
    f2 e[5] = {{s0[0], s0[1]}, {s0[2], s0[3]}, {s1[0], s1[1]}, {s1[2], s1[3]}, {sa[0], sa[1]}};
    const f2 m2 = {mx, mx};
    u32 pp[5];
#pragma unroll
    for (int i = 0; i < 5; i++) asm volatile("v_pk_add_f32 %0, %0, %1 op_sel_hi:[1,0] neg_lo:[0,1] neg_hi:[0,1]" : "+v"(e[i]) : "v"(m2));
#pragma unroll
    for (int i = 0; i < 4; i++) pp[i] = pk(__builtin_amdgcn_exp2f(e[i][0]), __builtin_amdgcn_exp2f(e[i][1]));
    const u4 pf = {pp[0], pp[1], pp[2], pp[3]};
    f4 o, sum;
    float e8, e9;
    asm volatile("v_exp_f32 %2, %7\n\ts_nop 0\n\tv_mfma_f32_16x16x32_bf16 %0, %4, %6, 0\n\tv_exp_f32 %3, %8\n\t"
                 "v_mfma_f32_16x16x32_bf16 %1, %5, %6, 0"
                 : "=&v"(o), "=&v"(sum), "=&v"(e8), "=&v"(e9) : "v"(vx), "v"(ones), "v"(pf), "v"(e[4][0]), "v"(e[4][1]));
    pm[0] = cvt(e8, e9);
    vx[0] = vd;
    asm volatile("s_nop 1\n\tv_mfma_f32_16x16x32_bf16 %0, %2, %4, %0\n\tv_mfma_f32_16x16x32_bf16 %1, %3, %4, %1"
                 : "+v"(sum), "+v"(o) : "v"(ones), "v"(vx), "v"(pm));
#pragma unroll
    for (int t = 4; t < 7; t++) d[t] = zero4();
    float inv;
    PINV(sum);
    asm volatile("s_nop 1\n\tv_rcp_f32 %0, %2\n\ts_nop 0" : "=v"(inv), "+v"(o) : "v"(sum[0]));
    *ow = u2{pk(o[0] * inv, o[1] * inv), pk(o[2] * inv, o[3] * inv)};
    SB();
    d[7] = zero4();
    if (B4W) VMW(2);
    BAR();
    TS(13);
    const u4 of = *(u4*)(sh + 20736 + l * 16);
    SB();
    asm volatile("s_sub_i32 s96, s96, %0\n\ts_add_i32 %0, %0, %2\n\ts_cmp_gt_i32 s96, " STR(XT) "\n\ts_cselect_b32 s97, " STR(XQ) ", 0\n\ts_add_i32 %0, %0, s97\n\t"
                 "s_cmp_gt_i32 s96, " STR(XT) "+" STR(XQ) "\n\ts_cselect_b32 s97, " STR(XQ) ", 0\n\ts_add_i32 %0, %0, s97"
                 : "+s"(tk), "+{s[96:97]}"(tb) : "s"(xa) : "scc");
    u4 pa, pb;
    f2 x2, x3;
#pragma unroll
    for (int t = 0; t < 8; t++) {
      SMV(d[t], of, wo[t]);
      if (t == 1 || t == 2) NOP(2);
      if (t == 3) x0 = fold(d[0]);
      if (t == 4) x1 = fold(d[1]);
      if (t == 5) pa[0] = cvt(x0[0], x1[0]);
      if (t == 6) pa[2] = cvt(x0[1], x1[1]);
    }
    x0 = fold(d[2]), x1 = fold(d[3]);
    pa[1] = cvt(x0[0], x1[0]), pa[3] = cvt(x0[1], x1[1]);
    SB();
    st16u<SC1>(pa, R, pv0, si);
    SB();
    x0 = fold(d[4]), x1 = fold(d[5]);
    asm volatile("" ::"v"(pa));
    x2 = fold(d[6]), pb[0] = cvt(x0[0], x1[0]), x3 = fold(d[7]);
    pb[2] = cvt(x0[1], x1[1]), pb[1] = cvt(x2[0], x3[0]), pb[3] = cvt(x2[1], x3[1]);
    st16u<SC1>(pb, R, pv1, si);
    SB();
    TS(4);
    u16v ra, rb;
    asm volatile("s_cmp_lg_u32 %[w], 0\n\ts_cbranch_scc1 9f\n\t" SLOT(EA) LDA "9:"
                 : RREGS("=&"), [t] "+s"(tk), [d] "=&s"(td)
                 : [w] "s"(w), [lo] "v"(l * 16), [rd] "s"(RD), [a0] "s"(sa0), [a1] "s"(sa1) : "memory", "s98", "s99", "scc");
    TS(5);
    PRE(s + 1);
    asm volatile("s_cmp_lg_u32 %[w], 0\n\ts_cbranch_scc0 1f\n\ts_waitcnt vmcnt(0)\n\ts_branch 9f\n"
                 "2:\n\ts_sub_i32 %[b], %[b], 1\n\ts_cmp_gt_i32 %[b], 0\n\ts_cbranch_scc0 3f\n\t" SLOT(XH) LDA "1:\n\t" RED
                 "v_cmp_eq_u32 vcc, -1, v252\n\ts_cbranch_vccnz 2b\n3:\n\ts_nop 3\n\t"
                 "v_add_f32_dpp v248, v248, v248 row_ror:8 row_mask:0xf bank_mask:0xf\n\tv_add_f32_dpp v249, v249, v249 row_ror:8 row_mask:0xf bank_mask:0xf\n\t"
                 "v_add_f32_dpp v250, v250, v250 row_ror:8 row_mask:0xf bank_mask:0xf\n\tv_add_f32_dpp v251, v251, v251 row_ror:8 row_mask:0xf bank_mask:0xf\n\t"
                 "v_cvt_pk_bf16_f32 v252, v248, v249\n\tv_cvt_pk_bf16_f32 v253, v250, v251\n\tv_min_u32 v252, -2, v252\n\tv_min_u32 v253, -2, v253\n\t"
                 "buffer_store_dwordx2 v[252:253], %[bo], %[rd], %[sb] offen sc1\n\t" DBT "s_add_i32 %[t], %[t], %[x]\n\t" SLOT(EB) "9:"
                 : RREGS("+"), [t] "+s"(tk), [d] "=&s"(td), [b] "+s"(budget)
                 : [w] "s"(w), [x] "s"(xb), [lo] "v"(l * 16), [rd] "s"(RD), [a0] "s"(sr), [a1] "s"(sr2), [sel] "v"(selB), [bo] "v"(bvo), [sb] "s"(sb)
                 : "memory", "s96", "s97", "s98", "s99", "scc", "vcc", "v248", "v249", "v250", "v251", "v252", "v253");
  }
  VM0();
  BAR();
  if (tid == 0) st4<SC1>(64, R, 0, W_HF + pid * 128);
  RT(pid == 0, 1);
}

DEV void tcu(rs_t R, u4 RD, u4 WS, u4 WQ, u4 WK, u4 WV, rs_t WO, rs_t X, char* sh, int h, int par, int w, int l) {
  u4* xt = (u4*)(sh + 131072);
  const int g = l >> 4, n = l & 15, tid = w * 64 + l;
  const f4 z4 = {0, 0, 0, 0};
  {
    for (int i = 0; i < par * TDL; i++) __builtin_amdgcn_s_sleep(32);
    u4 wq[32], wk[32], wv[32];
#define TT(i)
    const u32 lb = (u32)(uintptr_t)(__attribute__((address_space(3))) char*)sh;
    const u32 gv = (w * 4 + g) * 8192 + ((n ^ (4 * g)) * 16);
    const u32 ta = lb + (8 * g + (n >> 2)) * 256 + (((4 * w + (n & 3)) ^ (n & 12)) * 16);
#define XLOAD(seg)                                                                                \
  {                                                                                               \
    u4 t[8];                                                                                      \
    _Pragma("unroll") for (int i = 0; i < 8; i++) t[i] = ld16<0>(R, tid * 16, W_XN + (seg) * 32768 + i * 4096); \
    _Pragma("unroll") for (int i = 0; i < 8; i++) xt[i * 256 + tid] = t[i];                       \
  }
#define WREAD(m, arr, PIN)                                                 \
  _Pragma("unroll") for (int i = 0; i < 16; i++) {                         \
    const u32 a = ta + ((m) & 1) * 65536 + (i >> 1) * 8192 + (i & 1) * 8;  \
    const u2 t0 = trd(a), t1 = trd(a + 1024);                              \
    arr[((m) & 1) * 16 + i] = u4{t0[0], t0[1], t1[0], t1[1]};              \
  }                                                                        \
  _Pragma("unroll") for (int i = 0; i < 16; i++) PIN(arr[((m) & 1) * 16 + i]);
#define KVPASS(j)                                                                                  \
  {                                                                                                \
    f4 ak[4] = {zero4(), zero4(), zero4(), zero4()}, av[4] = {zero4(), zero4(), zero4(), zero4()}; \
    u4 xa = xt[l], xb = xt[64 + l]; NOP(1);                                    \
    _Pragma("unroll") for (int ks = 0; ks < 16; ks++) {                                            \
      const u4 x0 = xa, x1 = xb; \
      if (ks < 15) xa = xt[ks * 128 + 128 + l], xb = xt[ks * 128 + 192 + l]; \
      SB();                             \
      _Pragma("unroll") for (int t = 0; t < 2; t++) {                                              \
        MWA(ak[t], wk[2 * ks + t], x0); MWA(ak[2 + t], wk[2 * ks + t], x1);      \
        MMA(av[t], x0, wv[2 * ks + t]); MMA(av[2 + t], x1, wv[2 * ks + t]);      \
      }                                                                                            \
    }                                                                                              \
    WAIT4(ak); WAIT4(av); \
    const u32 so = ((4 * (j) + par) * 32 + h) * 8192 + w * 2048;                                   \
    _Pragma("unroll") for (int u = 0; u < 2; u++) {                                                \
      const f4 a = ak[2 * u], b = ak[2 * u + 1], c = av[u], d = av[2 + u];                         \
      st16<SC1>(u4{pk(a[0], b[0]), pk(a[1], b[1]), pk(a[2], b[2]), pk(a[3], b[3])}, R, l * 16, W_K + so + u * 1024); \
      st16<SC1>(u4{pk(c[0], c[1]), pk(c[2], c[3]), pk(d[0], d[1]), pk(d[2], d[3])}, R, l * 16, W_V + so + u * 1024); \
    }                                                                                              \
  }
    stage(WK, lb + w * 1024, gv, h * 256, 16 * 8192, 32 * 8192);
    stage(WK, lb + 65536 + w * 1024, gv, h * 256 + 2 * MB, 16 * 8192, 32 * 8192);
#pragma unroll
    for (int m = 0; m < 4; m++) {
      if (m == 1) { XLOAD(par); } else VMW(16);
      BAR();
      if (m < 2) { WREAD(m, wk, PINA); } else { WREAD(m, wv, PINV); }
      BAR();
      stage(m < 2 ? WV : WQ, lb + (m & 1) * 65536 + w * 1024, gv, h * 256 + (m & 1) * 2 * MB, 16 * 8192, 32 * 8192);
    }
    TT(0);
    KVPASS(0);
    VM0();
    BAR();
    if (tid == 0) st4<SC1>(1, R, par * 4, W_TF + h * 128);
    TT(1);
    WREAD(0, wq, PINA);
    WREAD(1, wq, PINA);
    BAR();
    for (int j = 0; j < 16; j++) {
      xt = (u4*)(sh + ((j & 1) ? 0 : 131072));
#pragma unroll
      for (int i = 0; i < 8; i++)
        asm volatile("s_mov_b32 m0, %0\n\ts_nop 0\n\tbuffer_load_dwordx4 %1, %2, %3 offen lds" ::"s"(lb + ((j & 1) ? 131072 : 0) + i * 4096 + w * 1024),
                     "v"(l * 16), "s"(RD), "s"(W_XN + (4 * min(j + 1, 15) + par) * 32768 + i * 4096 + w * 1024) : "memory");
      if (j >= 2 && j < 10)
        asm volatile("s_mov_b32 m0, %0\n\ts_nop 0\n\tbuffer_load_dword %1, %2, %3 offen lds" ::"s"(lb + 65536), "v"(l < 16 ? l * 128 : 0x80000000u), "s"(WS),
                     "s"((((h * 4 + par) * 4 + w) * 8 + j - 2) * 2048) : "memory");
      if (j > 0) KVPASS(j);
      f4 aq[4] = {zero4(), zero4(), zero4(), zero4()};
      u4 xq[8];
#pragma unroll
      for (int ks = 0; ks < 19; ks++) {
        if (ks < 16) xq[(2 * ks) & 7] = xt[ks * 128 + l], xq[(2 * ks + 1) & 7] = xt[ks * 128 + 64 + l];
        SB();
        if (ks >= 3) {
#pragma unroll
          for (int t = 0; t < 2; t++) { MWA(aq[t], wq[2 * ks - 6 + t], xq[(2 * ks - 6) & 7]); MWA(aq[2 + t], wq[2 * ks - 6 + t], xq[(2 * ks - 5) & 7]); }
        }
        SB();
      }
      WAIT4(aq);
      VM0();
      BAR();
      if (tid == 0) st4<SC1>(j + 1, R, par * 4, W_TF + h * 128);
#pragma unroll
      for (int u = 0; u < 2; u++) {
        const f4 e = aq[2 * u], f = aq[2 * u + 1];
        st16<SC1>(u4{pk(e[0], f[0]), pk(e[1], f[1]), pk(e[2], f[2]), pk(e[3], f[3])}, R, l * 16,
                  W_Q + ((4 * j + par) * 32 + h) * 8192 + w * 2048 + u * 1024);
      }
      if (j == 1) TT(2);
      if (j == 15) TT(3);
    }
    VM0();
    BAR();
  }
  if (EXP & 4) return;
  const int uq = w & 1, eh = w >> 1;
  int budget = 1 << 16;
  u4 wo[32];
#pragma unroll
  for (int ks = 0; ks < 4; ks++) {
    u4 r[8];
#pragma unroll
    for (int j = 0; j < 8; j++) r[j] = ld16<0>(WO, g * 8192 + n * 16, (h * 128 + 32 * ks + j) * 1024 + w * 256);
#pragma unroll
    for (int t = 0; t < 8; t++) {
#pragma unroll
      for (int j = 0; j < 4; j++) wo[ks * 8 + t][j] = (t & 1) ? phi(r[2 * j][t >> 1], r[2 * j + 1][t >> 1]) : plo(r[2 * j][t >> 1], r[2 * j + 1][t >> 1]);
      PINA(wo[ks * 8 + t]);
    }
  }
  u4* ob = (u4*)sh;
  f4* red = (f4*)(sh + 8192);
  u4 qf[4], kx[8], vf[4];
  u32 fh = 0, fp = 0;
  const u32 sel = getsel(R, budget);
#define QKVLOAD(so)                                                        \
  _Pragma("unroll") for (int i = 0; i < 4; i++) {                          \
    qf[i] = ld16<QSC>(R, l * 16, W_Q + (so) + (2 * i + uq) * 1024);        \
    kx[2 * i] = ld16<QSC>(R, l * 16, W_K + (so) + (2 * i) * 1024);         \
    kx[2 * i + 1] = ld16<QSC>(R, l * 16, W_K + (so) + (2 * i + 1) * 1024); \
    vf[i] = ld16<QSC>(R, l * 16, W_V + (so) + (4 * eh + i) * 1024);        \
  }
  QKVLOAD((par * 32 + h) * 8192);
  for (int j = 0; j < 16; j++) {
    const int seg = 4 * j + par;
    const u32 sm_ = (seg * 32 + h) * 2048;
    u4 kf[4];
    u32 vm[4];
    f4 s0 = z4, s1 = z4, sa = z4;
#pragma unroll
    for (int i = 0; i < 4; i++) s0 = mm(kx[2 * i], qf[i], s0), s1 = mm(kx[2 * i + 1], qf[i], s1);
    const u4 xr = ld16<0>(X, l * 16, (seg * 32 + h) * 1024);
    waitall<SLS>(R, w, (l & 1) * 128, W_HF + h * 256, seg, fh, seg == 63, budget);
    RT(h == 0 && seg == 63, 2);
#pragma unroll
    for (int i = 0; i < 4; i++) {
      kf[i] = ld16<SC1>(R, (2 * (n >> 2) + (n & 1)) * 256 + g * 16, W_KM + sm_ + i * 64);
      vm[i] = ld4<SC1>(R, l * 4, W_VM + sm_ + (4 * eh + i) * 256);
    }
#pragma unroll
    for (int i = 0; i < 4; i++) sa = mm(kf[i], qf[i], sa);
    float p[10];
    const float sc = 0.08838834764831845f;
#pragma unroll
    for (int r = 0; r < 4; r++) {
      p[r] = (4 * g + r <= 16 * uq + n) ? s0[r] * sc : -1e9f;
      p[4 + r] = (16 + 4 * g + r <= 16 * uq + n) ? s1[r] * sc : -1e9f;
    }
    p[8] = sa[0] * sc, p[9] = sa[1] * sc;
    float mx = p[0], sum = 0;
#pragma unroll
    for (int i = 1; i < 10; i++) mx = fmaxf(mx, p[i]);
    mx = gmax(mx);
#pragma unroll
    for (int i = 0; i < 10; i++) sum += (p[i] = __builtin_amdgcn_exp2f((p[i] - mx) * 1.4426950408889634f));
    const float inv = __builtin_amdgcn_rcpf(gsum(sum));
    const u4 pf = {pk(p[0], p[1]), pk(p[2], p[3]), pk(p[4], p[5]), pk(p[6], p[7])};
    const u32 pm = pk(p[8], p[9]);
#pragma unroll
    for (int i = 0; i < 2; i++) {
      f4 a = mm16(vm[2 * i], pm, mm(vf[2 * i], pf, z4)) * inv, b = mm16(vm[2 * i + 1], pm, mm(vf[2 * i + 1], pf, z4)) * inv;
      ob[(uq * 4 + 2 * eh + i) * 64 + l] = u4{pk(a[0], a[1]), pk(a[2], a[3]), pk(b[0], b[1]), pk(b[2], b[3])};
    }
    BAR();
    const u32 pg = par * 2 + (j & 1), pb = W_P + pg * 2 * MB + ((sel >> pg) & 1) * 8192;
#pragma unroll
    for (int u = 0; u < 2; u++) {
      f4 d[8];
      u4 of[4];
#pragma unroll
      for (int ks = 0; ks < 4; ks++) of[ks] = ob[(u * 4 + ks) * 64 + l];
#pragma unroll
      for (int t = 0; t < 8; t++) d[t] = zero4();
      NOP(1);
#pragma unroll
      for (int i = 0; i < 32; i++) MWB(d[i & 7], of[i >> 3], wo[i]);
      WAIT4(d);
      asm volatile("s_nop 7" : "+v"(d[4]), "+v"(d[5]), "+v"(d[6]), "+v"(d[7]));
#pragma unroll
      for (int r = 0; r < 4; r++) {
        const u32 tok = 16 * u + 4 * g + r;
        st16<SC1>(u4{pk(d[0][r], d[1][r]), pk(d[2][r], d[3][r]), pk(d[4][r], d[5][r]), pk(d[6][r], d[7][r])}, R,
                  tok * 65536 + ((h + tok) & 24) * 2048 + ((h + tok) & 7) * 1024 + n * 16, pb + w * 256);
      }
    }
    VM0();
    BAR();
    if (tid == 0) st4<SC1>(j + 1, R, h * 4, W_PF + par * 256);
    RT(h == 0 && seg == 63, 3);
    if (j < 15) { QKVLOAD((seg * 32 + 128 + h) * 8192); }
    waitall<SLP>(R, w, (l & 31) * 4, W_PF + par * 256, j, fp, seg == 63, budget);
    RT(h == 0 && seg == 63, 4);
    u4 r[8];
#pragma unroll
    for (int i = 0; i < 8; i++) r[i] = ld16<SC1>(R, l * 16, pb + h * 65536 + w * 16384 + i * 1024);
    f4 a = z4, b = z4;
#pragma unroll
    for (int i = 0; i < 8; i++) {
      a += f4{lo(r[i][0]), hi(r[i][0]), lo(r[i][1]), hi(r[i][1])};
      b += f4{lo(r[i][2]), hi(r[i][2]), lo(r[i][3]), hi(r[i][3])};
    }
    red[w * 128 + l] = a, red[w * 128 + 64 + l] = b;
    BAR();
    if (w == 0) {
      a = (a + red[128 + l]) + (red[256 + l] + red[384 + l]);
      b = (b + red[192 + l]) + (red[320 + l] + red[448 + l]);
      const u4 hv = {pk(lo(xr[0]) + a[0], hi(xr[0]) + a[1]), pk(lo(xr[1]) + a[2], hi(xr[1]) + a[3]),
                     pk(lo(xr[2]) + b[0], hi(xr[2]) + b[1]), pk(lo(xr[3]) + b[2], hi(xr[3]) + b[3])};
      st16<SC1>(lnrow(hv), R, l * 16, half(W_HN, seg * 32 + h, sel));
      VM0();
      if (l == 0) st4<SC1>(j + 1, R, h * 4, W_NF + par * 256);
      st16<0>(hv, R, l * 16, W_H + (seg * 32 + h) * 1024);
    }
  }
}

DEV float gelu(float x) {
  float y = 0.7978845608028654f * (x + 0.044715f * x * x * x);
  return x * __builtin_amdgcn_rcpf(1.f + __builtin_amdgcn_exp2f(-2.8853900817779268f * y));
}
template <bool H2> DEV void zcu(rs_t R, rs_t WI, char* sh, int par, int q, int w, int l) {
  u4* xt = (u4*)sh;
  float* st = (float*)(sh + 40960);
  const int g = l >> 4, n = l & 15;
  const bool last = w < (q == 3 ? 2 : 3);
  u4 bw[48];
#pragma unroll
  for (int i = 0; i < 48; i++) {
    u32 e[8];
    const u32 T = (H2 ? 43 : 0) + q * 11 + w + (i < 32 || last ? 4 * (i >> 4) : 0);
#pragma unroll
    for (int j = 0; j < 8; j++) e[j] = ld2(WI, g * 8 * 2752 + n * 2, (32 * (i & 15) + j) * 2752 + T * 32);
    bw[i] = u4{plo(e[0], e[1]), plo(e[2], e[3]), plo(e[4], e[5]), plo(e[6], e[7])};
    if ((i & 3) == 3) { PINA(bw[i - 3]); PINA(bw[i - 2]); PINA(bw[i - 1]); PINA(bw[i]); }
  }
  int budget = 1 << 16;
  u32 fn = 0;
  const u32 sel = getsel(R, budget);
  RT(H2 && q == 0 && par == 3, 9);
  for (int j = 0; j < 16; j++) {
    const int seg = 4 * j + par;
    waitall<SLS>(R, w, (l & 31) * 4, W_NF + par * 256, j, fn, seg >= 61, budget);
    RT(H2 && q == 0 && seg == 63, 5);
    RT(H2 && q == 0 && seg == 3, 10);
    u4 hv[8];
#pragma unroll
    for (int i = 0; i < 8; i++) hv[i] = ld16<SC1>(R, l * 16, half(W_HN, seg * 32 + w * 8, sel) + i * 1024);
#pragma unroll
    for (int i = 0; i < 8; i++) xt[(l >> 2) * 129 + (w >> 1) * 64 + (l & 3) * 16 + (w & 1) * 8 + i] = hv[i];
    f4 acc[6];
#pragma unroll
    for (int i = 0; i < 6; i++) acc[i] = zero4();
    BAR();
    u4 xq[8];
#pragma unroll
    for (int ks = 0; ks < 18; ks++) {
      if (ks < 16) xq[(2 * ks) & 7] = xt[ks * 129 + l], xq[(2 * ks + 1) & 7] = xt[ks * 129 + 64 + l];
      SB();
      if (ks >= 2) {
#pragma unroll
        for (int i = 0; i < 6; i++) {
          if (H2) MWB(acc[i], xq[(2 * ks - 4 + (i & 1)) & 7], bw[(i >> 1) * 16 + ks - 2]);
          else MWA(acc[i], bw[(i >> 1) * 16 + ks - 2], xq[(2 * ks - 4 + (i & 1)) & 7]);
        }
      }
      SB();
    }
    WAIT4(acc);
    asm volatile("s_nop 7" : "+v"(acc[4]), "+v"(acc[5]));
#pragma unroll
    for (int i = 0; i < 3; i++) {
#pragma unroll
      for (int r = 0; r < 8; r++) acc[2 * i + (r >> 2)][r & 3] = gelu(acc[2 * i + (r >> 2)][r & 3]);
      const f4 a = acc[2 * i], c = acc[2 * i + 1];
      if (i < 2 || last)
        st16<SC1>(u4{pk(a[0], a[1]), pk(a[2], a[3]), pk(c[0], c[1]), pk(c[2], c[3])}, R, l * 16,
                  H2 ? half(W_Z2, seg * 44 + q * 11 + w + 4 * i, sel) : W_Z1 + (seg * 44 + q * 11 + w + 4 * i) * 1024);
    }
    if (H2) {
#pragma unroll
      for (int p = 0; p < 4; p++) {
        const int u = p & 1;
        f4 s = p & 2 ? acc[u] * acc[u] + acc[2 + u] * acc[2 + u] : acc[u] + acc[2 + u];
        if (last) s += p & 2 ? acc[4 + u] * acc[4 + u] : acc[4 + u];
#pragma unroll
        for (int r = 0; r < 4; r++) s[r] = rsum(s[r]);
        if (n == 0) *(f4*)&st[w * 64 + p * 16 + g * 4] = s;
      }
      BAR();
      if (w == 0) st4<SC1>(fu((st[l] + st[64 + l]) + (st[128 + l] + st[192 + l])), R, l * 4, half(W_Z2, seg * 44 + 43, sel) + q * 256);
    }
    VM0();
    BAR();
    if (w == 0 && l == 0) st4<SC1>(seg + 4, R, ((H2 ? 4 : 0) + q) * 4, W_ZF + par * 256);
    RT(H2 && q == 0 && seg == 63, 6);
  }
}

DEV void scu(rs_t R, rs_t S, rs_t BS, char* sh, int m, int q, int w, int l) {
  u4* zb = (u4*)sh;
  const int g = l >> 4, n = l & 15, tt = m * 16 + w * 4, nt = q == 3 ? 10 : 11;
  f4 acc[44];
  u4 z1[22];
  u2 an[16];
  float bs[4];
#pragma unroll
  for (int i = 0; i < 4; i++) bs[i] = lo(ld2(BS, n * 2, (tt + i) * 32));
#pragma unroll
  for (int i = 0; i < 44; i++) acc[i] = f4{bs[i & 3], bs[i & 3], bs[i & 3], bs[i & 3]};
#pragma unroll
  for (int i = 0; i < 22; i++) z1[i] = u4{0, 0, 0, 0};
#pragma unroll
  for (int i = 0; i < 16; i++) an[i] = ld8<0>(S, n * 4096 + g * 8 + (i & 3) * 32, (tt + (i >> 2)) * 65536);
  int budget = 1 << 16;
  u32 fz = 0;
  const u32 sel = getsel(R, budget);
  for (int s = 0; s < 64; s += 2) {
    waitall<SLS>(R, w, (l & 7) * 4 + (l & 24) * 32, W_ZF, s + 1, fz, s >= 60, budget);
    RT(m == 7 && q == 0 && s == 62, 7);
    RT(m == 7 && q == 0 && (s & 15) == 0, 11 + (s >> 4));
    u4 z[6], a[8], sv[2];
#pragma unroll
    for (int y = 0; y < 2; y++) {
      sv[y] = ld16<SC1>(R, (n & 3) * 256 + (n >> 2) * 64 + g * 16, half(W_Z2, (s + y) * 44 + 43, sel));
#pragma unroll
      for (int i = 0; i < 3; i++) z[y * 3 + i] = ld16<SC1>(R, l * 16, half(W_Z2, (s + y) * 44 + q * 11 + min(w + 4 * i, nt - 1), sel));
    }
#pragma unroll
    for (int i = 0; i < 8; i++) a[i] = u4{an[2 * i][0], an[2 * i][1], an[2 * i + 1][0], an[2 * i + 1][1]};
    SB();
#pragma unroll
    for (int i = 0; i < 16; i++) an[i] = ld8<0>(S, n * 4096 + g * 8 + (i & 3) * 32, (tt + (i >> 2)) * 65536 + min(s + 2, 62) * 64);
    SB();
#pragma unroll
    for (int y = 0; y < 2; y++) {
      float rs[8], nb[8];
#pragma unroll
      for (int r = 0; r < 4; r++) {
        float v = uf(sv[y][r]);
        v += dpp<0xB1>(v);
        v += dpp<0x4E>(v);
        v *= 1.f / 688;
        const float e = __builtin_amdgcn_rsqf(fmaxf(dpp<0x108>(v) - v * v, 0.f) + 1e-5f), c = -v * e;
        rs[r] = dpp<0x150>(e), rs[4 + r] = dpp<0x154>(e), nb[r] = dpp<0x150>(c), nb[4 + r] = dpp<0x154>(c);
      }
#pragma unroll
      for (int i = 0; i < 3; i++) {
        u4 o;
#pragma unroll
        for (int r = 0; r < 4; r++) o[r] = pk(lo(z[y * 3 + i][r]) * rs[2 * r] + nb[2 * r], hi(z[y * 3 + i][r]) * rs[2 * r + 1] + nb[2 * r + 1]);
        zb[(y * 12 + w + 4 * i) * 64 + l] = o;
      }
    }
    if (s == (tt >> 1)) {
#pragma unroll
      for (int i = 0; i < 22; i++) z1[i] = ld16<SC1>(R, l * 16, W_Z1 + ((s + (i & 1)) * 44 + q * 11 + min(i >> 1, nt - 1)) * 1024);
    }
    BAR();
#pragma unroll
    for (int i = 0; i < 22; i++) {
      const u4 zf = zb[((i & 1) * 12 + (i >> 1)) * 64 + l];
#pragma unroll
      for (int t = 0; t < 4; t++) acc[(i >> 1) * 4 + t] = mm(zf, a[2 * t + (i & 1)], acc[(i >> 1) * 4 + t]);
    }
  }
#pragma unroll
  for (int i = 0; i < 24; i++) {
    const int t = i & 3, p = i >> 2;
    u4 o = {0, 0, 0, 0};
#pragma unroll
    for (int y = 0; y < (p < 5 ? 2 : 1); y++) {
      const f4 s = acc[(2 * p + y) * 4 + t];
      const u4 z = z1[(2 * p + y) * 2 + (t >> 1)];
      const u32 z0 = z[2 * (t & 1)], zz = z[2 * (t & 1) + 1];
      o[2 * y] = pk(lo(z0) * s[0], hi(z0) * s[1]), o[2 * y + 1] = pk(lo(zz) * s[2], hi(zz) * s[3]);
    }
    if (p == 5 && q == 3) o[0] = o[1] = 0;
    st16<0>(o, R, l * 16, W_U + ((tt + t) * 24 + 6 * q + p) * 1024);
  }
  VM0();
  RT(m == 7 && q == 0, 8);
}

__global__ __launch_bounds__(256, 1) void k1(const void* wq, const void* wk, const void* wv, const void* wo, const void* win,
                                              const void* wsp, const void* wout, const void* xin, const void* bsp, void* ws) {
  __shared__ __attribute__((aligned(16))) char sh[163840];
  const int b = blockIdx.x, x = b & 7, i = b >> 3, l = threadIdx.x & 63;
  const int w = __builtin_amdgcn_readfirstlane(threadIdx.x >> 6);
  rs_t R = mkws(ws);
  RT(b == 0, 0);
  const u4 RD = desc((void*)(((uintptr_t)ws + (2 * MB - 1)) & ~(uintptr_t)(2 * MB - 1)), W_END);
  if (x >= 4) {
    tcu(R, RD, desc(wsp, 8 * MB), desc(wq, 4 * MB), desc(wk, 4 * MB), desc(wv, 4 * MB), mk(wo, 4 * MB), mk(xin, 2 * MB), sh, (x - 4) * 8 + (i >> 2), i & 3, w, l);
  } else if (i < 16) {
    hcu(R, RD, desc(wq, 4 * MB), desc(wk, 4 * MB), desc(wv, 4 * MB), mk(wo, 4 * MB), sh, x * 8 + (i >> 1), i & 1, w, l);
  } else {
    if (b == 128) {
      if (w == 0) {
        u32 best[4] = {~0u, ~0u, ~0u, ~0u};
        for (int t = 0; t < 4; t++) {
#pragma unroll
          for (int p = 0; p < 4; p++) {
            const u32 c0 = (u32)__builtin_readcyclecounter();
            st4<SC1>(SENT, R, (l & 31) * 4 + t * 1792, (p >> 1) * (2 * MB) + 65536 + (p & 1) * 8192);
            VM0();
            best[p] = min(best[p], (u32)__builtin_readcyclecounter() - c0);
          }
        }
        if (l == 0) st8<SC1>(u2{1u | (best[1] < best[0] ? 2u : 0u) | (best[3] < best[2] ? 4u : 0u), (u32)__builtin_amdgcn_s_memrealtime()}, R, 0, W_SEL);
      }
      __syncthreads();
    } else if (b == 192) {
      if (w == 0) {
        u32 sel = 1 << 16;
        for (int p = 0; p < 13; p++) {
          u32 best[2] = {~0u, ~0u};
          for (int t = 0; t < 4; t++) {
#pragma unroll
            for (int y = 0; y < 2; y++) {
              const u32 c0 = (u32)__builtin_readcyclecounter();
              st4<SC1>(SENT, R, (l & 31) * 4 + t * 1792, W_P + p * (2 * MB) + y * 8192);
              VM0();
              best[y] = min(best[y], (u32)__builtin_readcyclecounter() - c0);
            }
          }
          sel |= (((PNEAR >> p) & 1) ? best[1] < best[0] : best[1] > best[0]) ? 1u << p : 0u;
        }
        if (l == 0) st4<SC1>(sel, R, 0, W_SEL + 512);
      }
      __syncthreads();
    } else {
      for (int t = 0; t < 2; t++) __builtin_amdgcn_s_sleep(127);
    }
    if (i < 24) {
      if (EXP & 2) return;
      const rs_t WI = mk(win, 512 * 2752);
      if (x & 2) zcu<true>(R, WI, sh, (i - 16) >> 1, (x & 1) * 2 + (i & 1), w, l);
      else zcu<false>(R, WI, sh, (i - 16) >> 1, (x & 1) * 2 + (i & 1), w, l);
    } else {
      const rs_t S = mk(wout, 688 * 1024);
      for (int f = ((i - 24) * 4 + x) * 4 + w; f < 768; f += 128) {
        const int T = f / 24, ks = f - T * 24, kq = ks / 6, p = ks - kq * 6;
        u32 e[8];
#pragma unroll
        for (int j = 0; j < 8; j++)
          e[j] = ld2(S, (16 * (p == 5 && j >= 4 ? 64 : 11 * kq + 2 * p + (j >> 2)) + 4 * (l >> 4) + (j & 3)) * 1024 + (16 * T + (l & 15)) * 2, 0);
        st16<0>(u4{plo(e[0], e[1]), plo(e[2], e[3]), plo(e[4], e[5]), plo(e[6], e[7])}, R, l * 16, W_WU + f * 1024);
      }
      if (!(EXP & 1)) scu(R, mk(wsp, 8 * MB), mk(bsp, 4096), sh, 2 * x + ((i >> 2) & 1), i & 3, w, l);
    }
  }
}
template <int MT, int NT, int NI, int PF, bool TAIL>
DEV void gloop(rs_t R, u32 v, const u32 (&ao)[MT], const u32 (&bo)[NT], u32 step, bool last, f4 (&acc)[MT][NT]) {
  u4 a[PF][MT], b[PF][NT];
#pragma unroll
  for (int i = 0; i < NI + PF - 1; i++) {
    if (i < NI) {
      u32 o = (TAIL && i == NI - 1 && !last) ? 0 : i * step;
#pragma unroll
      for (int m = 0; m < MT; m++) a[i % PF][m] = ld16<0>(R, v, ao[m] + o);
#pragma unroll
      for (int n = 0; n < NT; n++) b[i % PF][n] = ld16<0>(R, v, bo[n] + o);
    }
    SB();
    int j = i - (PF - 1);
    if (j >= 0 && (!TAIL || j < NI - 1 || last)) {
#pragma unroll
      for (int m = 0; m < MT; m++)
#pragma unroll
        for (int n = 0; n < NT; n++) acc[m][n] = mm(b[j % PF][n], a[j % PF][m], acc[m][n]);
    }
    SB();
  }
}

template <int NKS, u32 AOFF, u32 BOFF, bool FINAL>
__global__ __launch_bounds__(256) void kres(const void* x, void* out, void* ws) {
  __shared__ f4 red[4][16][64];
  const int tid = threadIdx.x, b = blockIdx.x, l = tid & 63;
  const int w = __builtin_amdgcn_readfirstlane(tid >> 6);
  rs_t R = mkws(ws);
  const int mb = (b & 7) * 4 + ((b >> 3) & 3), nb = b >> 5;
  u32 ao[4], bo[4];
  f4 acc[4][4];
#pragma unroll
  for (int i = 0; i < 4; i++) {
    ao[i] = AOFF + ((mb * 4 + i) * NKS + w) * 1024;
    bo[i] = BOFF + ((nb * 4 + i) * NKS + w) * 1024;
#pragma unroll
    for (int j = 0; j < 4; j++) acc[i][j] = f4{0, 0, 0, 0};
  }
  u32 ro = ((mb * 4 + w) * 16 + (l & 15)) * 1024 + (nb * 64 + (l >> 4) * 4) * 2;
  u2 rv[4];
#pragma unroll
  for (int j = 0; j < 4; j++) rv[j] = FINAL ? ld8<0>(R, ro + j * 32, W_H) : ld8<0>(mk(x, 2 * MB), ro + j * 32, 0);
  gloop<4, 4, (NKS + 3) / 4, 4, (NKS & 3) != 0>(R, l * 16, ao, bo, 4096, w < (NKS & 3), acc);
#pragma unroll
  for (int i = 0; i < 16; i++) red[w][i][l] = acc[i >> 2][i & 3];
  __syncthreads();
#pragma unroll
  for (int j = 0; j < 4; j++) {
    f4 s = (red[0][w * 4 + j][l] + red[1][w * 4 + j][l]) + (red[2][w * 4 + j][l] + red[3][w * 4 + j][l]);
    u2 o = {pk(lo(rv[j][0]) + s[0], hi(rv[j][0]) + s[1]), pk(lo(rv[j][1]) + s[2], hi(rv[j][1]) + s[3])};
    if (FINAL) st8<0>(o, mk(out, 2 * MB), ro + j * 32, 0);
    else st8<0>(o, R, ro + j * 32, W_H);
  }
}

extern "C" void kernel_launch(void* const* d_in, const int* in_sizes, int n_in, void* d_out, int out_size, void* d_ws,
                              size_t ws_size, hipStream_t stream) {
  k0<<<256, 256, 0, stream>>>(d_in[0], d_in[1], d_in[2], d_in[3], d_in[4], d_in[5], d_in[8], d_in[11], d_ws);
  k1<<<256, 256, 0, stream>>>(d_in[2], d_in[3], d_in[4], d_in[5], d_in[8], d_in[9], d_in[11], d_in[0], d_in[10], d_ws);
  if (DBG == 3) return;
  kres<24, W_U, W_WU, true><<<256, 256, 0, stream>>>(d_in[0], d_out, d_ws);
}
```
